```python
import torch

def _softmax(x, axis=-1):
    return torch.softmax(x, dim=axis)

def _gelu(x):
    return torch.nn.functional.gelu(x, approximate='tanh')

def _silu(x):
    return torch.nn.functional.silu(x)

def _sigmoid(x):
    return torch.sigmoid(x)

def _layernorm(x, axis=-1, eps=1e-05):
    m = x.mean(dim=axis, keepdim=True)
    v = x.var(dim=axis, keepdim=True, unbiased=False)
    return (x - m) / torch.sqrt(v + eps)

def _scatter_add(tgt, idx, src, axis=0):
    return tgt.clone().index_add_(axis, idx, src)

def _segment_sum(src, seg, n):
    out = torch.zeros((n,) + src.shape[1:], dtype=src.dtype)
    return out.index_add_(0, seg, src)

def _pad(x, pw):
    flat = tuple((v for pair in reversed(pw) for v in pair))
    return torch.nn.functional.pad(x, flat)
kernel_name = 'gather_scatter__tensor_product_equiv_mace__graph'

def setup_inputs():

    _w = lambda a: (lambda t: t.to(torch.bfloat16) if t.is_floating_point() else t)(torch.as_tensor(a))
    N, deg = (50000, 16)
    Lmax, C, De = (1, 64, 4)
    M = (Lmax + 1) ** 2
    E = N * deg
    src = (lambda lo, hi, *s: torch.randint(lo, hi, s))(0, N, E).to(torch.int64)
    dst = (lambda lo, hi, *s: torch.randint(lo, hi, s))(0, N, E).to(torch.int64)
    r = (lambda *s: torch.randn(*s, dtype=torch.bfloat16) * 0.05)(E, 3).to(float)
    r = r / (torch.linalg.norm(r, dim=-1, keepdim=True).to(float) + 1e-06)
    rx, ry, rz = (r[:, 0].clone(), r[:, 1].clone(), r[:, 2].clone())
    x2, y2, z2 = (rx * rx, ry * ry, rz * rz)
    sh_list = [torch.ones_like(rx), rx, ry, rz, rx * ry, ry * rz, 2.0 * z2 - x2 - y2, rx * rz, x2 - y2, ry * (3.0 * x2 - y2), rx * ry * rz, ry * (4.0 * z2 - x2 - y2), rz * (2.0 * z2 - 3.0 * x2 - 3.0 * y2), rx * (4.0 * z2 - x2 - y2), rz * (x2 - y2), rx * (x2 - 3.0 * y2)]
    sh16 = torch.stack(sh_list, dim=-1).to(float)
    pad = max(0, M - 16)
    sh_full = torch.cat([sh16, torch.zeros((E, pad), dtype=float)], dim=-1)[:, :M]
    cg = ((lambda *s: torch.randn(*s, dtype=torch.bfloat16) * 0.05)(M, M, M) * (1.0 / float(M))).to(float)
    return {'x': _w(((lambda *s: torch.randn(*s, dtype=torch.bfloat16) * 0.05)(N, M, C) * 0.3).to(float)), 'src': _w(src), 'dst': _w(dst), 'sh': _w(sh_full.to(float)), 'edge_attr': _w(((lambda *s: torch.randn(*s, dtype=torch.bfloat16) * 0.05)(E, De) * 0.3).to(float)), 'cg': _w(cg), 'w_r': _w(((lambda *s: torch.randn(*s, dtype=torch.bfloat16) * 0.05)(De, C) * (1.0 / float(De) ** 0.5)).to(float)), 'w_out': _w(((lambda *s: torch.randn(*s, dtype=torch.bfloat16) * 0.05)(C, C) * (1.0 / float(C) ** 0.5)).to(float))}

def reference(x, src, dst, sh, edge_attr, cg, w_r, w_out):
    N = x.shape[0]
    M = x.shape[1]
    C = x.shape[2]
    h = x[src]
    tp = torch.einsum('uvw,euc,ev->ewc', cg, h, sh)
    rw = edge_attr @ w_r
    msg = tp * rw[:, None, :]
    msg2 = msg.reshape(-1, M * C)
    agg = _segment_sum(msg2, dst, N).reshape(N, M, C)
    out = torch.einsum('nmc,cd->nmd', agg, w_out)
    return out
```

```cpp
#include <hip/hip_runtime.h>
#include <hip/hip_bf16.h>
#include <stdint.h>

typedef __attribute__((ext_vector_type(8))) short bf16x8;
typedef __attribute__((ext_vector_type(4))) float f32x4;
typedef __attribute__((ext_vector_type(4))) unsigned u32x4;
typedef __attribute__((ext_vector_type(2))) __bf16 bf16x2_t;

#define MDIM 4
#define CDIM 64
#define ROWE 256

static __device__ __forceinline__ unsigned pk_bf16(float a, float b) {
  bf16x2_t v;
  v.x = (__bf16)a;
  v.y = (__bf16)b;
  return __builtin_bit_cast(unsigned, v);
}
static __device__ __forceinline__ float bf_lo(unsigned v) { return __uint_as_float(v << 16); }
static __device__ __forceinline__ float bf_hi(unsigned v) { return __uint_as_float(v & 0xffff0000u); }

__global__ __launch_bounds__(1024) void k_hist(const int* __restrict__ dst, int E, int chunk, int NB, int B,
                                               int* __restrict__ hist) {
  extern __shared__ __attribute__((aligned(16))) int s_h[];
  const int tid = threadIdx.x, T = blockDim.x, g = blockIdx.x;
  for (int i = tid; i < B; i += T) s_h[i] = 0;
  __syncthreads();
  const int lo = g * chunk;
  const int hi = min(E, lo + chunk);
  const int nvec = (hi - lo) >> 2;
  const int4* d4 = reinterpret_cast<const int4*>(dst + lo);
  for (int i = tid; i < nvec; i += T) {
    int4 d = d4[i];
    atomicAdd(&s_h[(unsigned)d.x / (unsigned)NB], 1);
    atomicAdd(&s_h[(unsigned)d.y / (unsigned)NB], 1);
    atomicAdd(&s_h[(unsigned)d.z / (unsigned)NB], 1);
    atomicAdd(&s_h[(unsigned)d.w / (unsigned)NB], 1);
  }
  for (int e = lo + nvec * 4 + tid; e < hi; e += T) atomicAdd(&s_h[(unsigned)dst[e] / (unsigned)NB], 1);
  __syncthreads();
  for (int i = tid; i < B; i += T) hist[(size_t)g * B + i] = s_h[i];
}

__global__ __launch_bounds__(1024) void k_scatter(const int* __restrict__ src, const int* __restrict__ dst,
                                                  const uint2* __restrict__ sh, const uint2* __restrict__ ea,
                                                  int E, int chunk, int NB, int B, int G,
                                                  const int* __restrict__ hist, int* __restrict__ bstart,
                                                  uint4* __restrict__ rec) {
  extern __shared__ __attribute__((aligned(16))) int s_m[];
  int* s_base = s_m;
  int* s_tot = s_m + B;
  int* s_wsum = s_m + 2 * B;
  const int tid = threadIdx.x, T = blockDim.x, g = blockIdx.x;
  const int lane = tid & 63, wv = tid >> 6, NW = T >> 6;
  for (int b = tid; b < B; b += T) {
    int tot = 0, pre = 0;
    for (int gg = 0; gg < G; ++gg) {
      int h = hist[(size_t)gg * B + b];
      tot += h;
      pre += (gg < g) ? h : 0;
    }
    s_tot[b] = tot;
    s_base[b] = pre;
  }
  __syncthreads();
  int carry = 0;
  for (int b0 = 0; b0 < B; b0 += T) {
    int b = b0 + tid;
    int v = (b < B) ? s_tot[b] : 0;
    int inc = v;
#pragma unroll
    for (int o = 1; o < 64; o <<= 1) {
      int t = __shfl_up(inc, o, 64);
      if (lane >= o) inc += t;
    }
    if (lane == 63) s_wsum[wv] = inc;
    __syncthreads();
    int woff = 0, total = 0;
    for (int w = 0; w < NW; ++w) {
      int t = s_wsum[w];
      woff += (w < wv) ? t : 0;
      total += t;
    }
    int excl = carry + woff + inc - v;
    if (b < B) {
      s_base[b] += excl;
      if (g == 0) bstart[b] = excl;
    }
    carry += total;
    __syncthreads();
  }
  if (g == 0 && tid == 0) bstart[B] = carry;
  const int lo = g * chunk;
  const int hi = min(E, lo + chunk);
  for (int e = lo + tid; e < hi; e += T) {
    int d = dst[e];
    int b = (unsigned)d / (unsigned)NB;
    int s = src[e];
    uint2 a = sh[e];
    uint2 c = ea[e];
    int pos = atomicAdd(&s_base[b], 1);
    uint4 r0 = make_uint4((unsigned)e, (unsigned)s, (unsigned)(d - b * NB), 0u);
    uint4 r1 = make_uint4(a.x, a.y, c.x, c.y);
    rec[(size_t)pos * 2] = r0;
    rec[(size_t)pos * 2 + 1] = r1;
  }
}

#define SCR_ROW 144
#define SCR_WAVE (16 * SCR_ROW)

template <int T, int CAP, int NBMAX>
struct MainLds {
  int off[NBMAX + 8];
  int elist[CAP];
  int srcS[CAP];
  uint4 seS[CAP];
  uint4 wo[8 * 64];
  char scr[(T / 64) * SCR_WAVE];
};


template <int T, int CAP, int NBMAX>
__global__ __launch_bounds__(T) void k_main(const uint16_t* __restrict__ x, const uint4* __restrict__ rec,
                                            const int* __restrict__ bstart, const uint16_t* __restrict__ cg,
                                            const uint16_t* __restrict__ w_r, const uint16_t* __restrict__ w_out,
                                            uint16_t* __restrict__ out, int N, int NB) {
  extern __shared__ __attribute__((aligned(16))) char smem[];
  typedef MainLds<T, CAP, NBMAX> L;
  L& S = *reinterpret_cast<L*>(smem);
  constexpr int NW = T / 64;
  constexpr int RPT = CAP / T;
  const int tid = threadIdx.x, lane = tid & 63;
  const int wv = __builtin_amdgcn_readfirstlane(tid >> 6);
  const int b = blockIdx.x;
  const int node0 = b * NB;
  const int nn = min(NB, N - node0);
  const int rbeg = bstart[b];
  const int nrec = bstart[b + 1] - rbeg;

  for (int idx = tid; idx < 512; idx += T) {
    int l = idx & 63, st = idx >> 6;
    int s = st >> 2, tp = st & 3;
    int rho = l & 15, gg = l >> 4;
    int d = 16 * (rho >> 2) + 4 * tp + (rho & 3);
    unsigned v[8];
#pragma unroll
    for (int j = 0; j < 8; ++j) v[j] = w_out[(32 * s + 8 * gg + j) * CDIM + d];
    S.wo[idx] = make_uint4(v[0] | (v[1] << 16), v[2] | (v[3] << 16), v[4] | (v[5] << 16), v[6] | (v[7] << 16));
  }
  for (int i = tid; i <= nn; i += T) S.off[i] = 0;
  __syncthreads();

  {
    u32x4 ra[RPT], rb[RPT];
    int slot[RPT];
    const u32x4* recv = reinterpret_cast<const u32x4*>(rec);
#pragma unroll
    for (int q = 0; q < RPT; ++q) {
      int r = tid + q * T;
      ra[q] = (u32x4){0, 0, 0, 0};
      rb[q] = (u32x4){0, 0, 0, 0};
      slot[q] = 0;
      if (r < nrec) {
        ra[q] = recv[(size_t)(rbeg + r) * 2];
        rb[q] = recv[(size_t)(rbeg + r) * 2 + 1];
      }
    }
#pragma unroll
    for (int q = 0; q < RPT; ++q) {
      int r = tid + q * T;
      if (r < nrec) slot[q] = atomicAdd(&S.off[ra[q].z], 1);
    }
    __syncthreads();
    if (wv == 0) {
      int c[4], sum = 0;
#pragma unroll
      for (int j = 0; j < 4; ++j) {
        int i = lane * 4 + j;
        c[j] = (i < nn) ? S.off[i] : 0;
        sum += c[j];
      }
      int inc = sum;
#pragma unroll
      for (int o = 1; o < 64; o <<= 1) {
        int t = __shfl_up(inc, o, 64);
        if (lane >= o) inc += t;
      }
      int run = inc - sum;
#pragma unroll
      for (int j = 0; j < 4; ++j) {
        int i = lane * 4 + j;
        if (i <= nn) S.off[i] = run;
        run += c[j];
      }
    }
    __syncthreads();
#pragma unroll
    for (int q = 0; q < RPT; ++q) {
      int r = tid + q * T;
      if (r < nrec) S.elist[S.off[ra[q].z] + slot[q]] = (int)ra[q].x;
    }
    __syncthreads();
#pragma unroll
    for (int q = 0; q < RPT; ++q) {
      int r = tid + q * T;
      if (r < nrec) {
        int o0 = S.off[ra[q].z], o1 = S.off[ra[q].z + 1];
        int e = (int)ra[q].x, rank = 0;
        for (int i = o0; i < o1; ++i) rank += (S.elist[i] < e) ? 1 : 0;
        S.srcS[o0 + rank] = (int)ra[q].y;
        *reinterpret_cast<u32x4*>(&S.seS[o0 + rank]) = rb[q];
      }
    }
    __syncthreads();
  }

  const int g = lane >> 4, li = lane & 15;
  const int wrow = li >> 2, krow = li & 3;
  float cgw[4][4];
#pragma unroll
  for (int u = 0; u < 4; ++u)
#pragma unroll
    for (int v = 0; v < 4; ++v) cgw[u][v] = __uint_as_float((unsigned)cg[(u * 4 + v) * 4 + wrow] << 16);
  float wr[4][4];
#pragma unroll
  for (int k = 0; k < 4; ++k)
#pragma unroll
    for (int t = 0; t < 4; ++t) wr[k][t] = __uint_as_float((unsigned)w_r[k * CDIM + 4 * li + t] << 16);

  const int nlo = (wv * nn) / NW, nhi = ((wv + 1) * nn) / NW;
  if (nlo >= nhi) return;
  char* scr = S.scr + wv * SCR_WAVE;

#define ISSUE(XV, SE0, SE1, POS, END)                                                        \
  {                                                                                          \
    const int i0_ = (POS) + 2 * g, i1_ = i0_ + 1;                                            \
    const bool v0_ = i0_ < (END), v1_ = i1_ < (END);                                         \
    SE0 = make_uint4(0, 0, 0, 0);                                                            \
    SE1 = make_uint4(0, 0, 0, 0);                                                            \
    _Pragma("unroll") for (int u = 0; u < 8; ++u) XV[u] = make_uint2(0, 0);                  \
    if (v0_) {                                                                               \
      SE0 = S.seS[i0_];                                                                      \
      const int s0_ = S.srcS[i0_];                                                           \
      const uint2* p_ = reinterpret_cast<const uint2*>(x + (size_t)s0_ * ROWE + 4 * li);     \
      _Pragma("unroll") for (int u = 0; u < 4; ++u) XV[u] = p_[u * 16];                      \
    }                                                                                        \
    if (v1_) {                                                                               \
      SE1 = S.seS[i1_];                                                                      \
      const int s1_ = S.srcS[i1_];                                                           \
      const uint2* p_ = reinterpret_cast<const uint2*>(x + (size_t)s1_ * ROWE + 4 * li);     \
      _Pragma("unroll") for (int u = 0; u < 4; ++u) XV[4 + u] = p_[u * 16];                  \
    }                                                                                        \
  }

  auto coef4 = [&](const uint4& se, unsigned& c01, unsigned& c23) {
    float s0 = bf_lo(se.x), s1 = bf_hi(se.x), s2 = bf_lo(se.y), s3 = bf_hi(se.y);
    unsigned ew = (krow & 2) ? se.w : se.z;
    float ek = (krow & 1) ? bf_hi(ew) : bf_lo(ew);
    float c[4];
#pragma unroll
    for (int u = 0; u < 4; ++u) {
      float t = cgw[u][0] * s0;
      t = fmaf(cgw[u][1], s1, t);
      t = fmaf(cgw[u][2], s2, t);
      t = fmaf(cgw[u][3], s3, t);
      c[u] = t * ek;
    }
    c01 = pk_bf16(c[0], c[1]);
    c23 = pk_bf16(c[2], c[3]);
  };

  f32x4 acc[4];
#pragma unroll
  for (int t = 0; t < 4; ++t) acc[t] = (f32x4){0.f, 0.f, 0.f, 0.f};

  int node = nlo;
  int pos = S.off[node], end = S.off[node + 1];
  pos = __builtin_amdgcn_readfirstlane(pos);
  end = __builtin_amdgcn_readfirstlane(end);
  uint2 cx[8], nx[8];
  uint4 cse0, cse1, nse0, nse1;
  ISSUE(cx, cse0, cse1, pos, end);
  int nb = 0;
  int batch0 = nlo;
  while (true) {
    int npos = pos + 8, nend = end, nnode = node;
    bool last = npos >= end;
    if (last) {
      nnode = node + 1;
      if (nnode < nhi) {
        npos = __builtin_amdgcn_readfirstlane(S.off[nnode]);
        nend = __builtin_amdgcn_readfirstlane(S.off[nnode + 1]);
      }
    }
    bool more = nnode < nhi;
    if (!more) nend = npos;
    ISSUE(nx, nse0, nse1, npos, nend);

    uint4 A;
    coef4(cse0, A.x, A.y);
    coef4(cse1, A.z, A.w);
    bf16x8 Af = __builtin_bit_cast(bf16x8, A);
    {
      uint4 B0, B1, B2, B3;
      B0.x = __builtin_amdgcn_perm(cx[1].x, cx[0].x, 0x05040100u);
      B0.y = __builtin_amdgcn_perm(cx[3].x, cx[2].x, 0x05040100u);
      B0.z = __builtin_amdgcn_perm(cx[5].x, cx[4].x, 0x05040100u);
      B0.w = __builtin_amdgcn_perm(cx[7].x, cx[6].x, 0x05040100u);
      B1.x = __builtin_amdgcn_perm(cx[1].x, cx[0].x, 0x07060302u);
      B1.y = __builtin_amdgcn_perm(cx[3].x, cx[2].x, 0x07060302u);
      B1.z = __builtin_amdgcn_perm(cx[5].x, cx[4].x, 0x07060302u);
      B1.w = __builtin_amdgcn_perm(cx[7].x, cx[6].x, 0x07060302u);
      B2.x = __builtin_amdgcn_perm(cx[1].y, cx[0].y, 0x05040100u);
      B2.y = __builtin_amdgcn_perm(cx[3].y, cx[2].y, 0x05040100u);
      B2.z = __builtin_amdgcn_perm(cx[5].y, cx[4].y, 0x05040100u);
      B2.w = __builtin_amdgcn_perm(cx[7].y, cx[6].y, 0x05040100u);
      B3.x = __builtin_amdgcn_perm(cx[1].y, cx[0].y, 0x07060302u);
      B3.y = __builtin_amdgcn_perm(cx[3].y, cx[2].y, 0x07060302u);
      B3.z = __builtin_amdgcn_perm(cx[5].y, cx[4].y, 0x07060302u);
      B3.w = __builtin_amdgcn_perm(cx[7].y, cx[6].y, 0x07060302u);
      acc[0] = __builtin_amdgcn_mfma_f32_16x16x32_bf16(Af, __builtin_bit_cast(bf16x8, B0), acc[0], 0, 0, 0);
      acc[1] = __builtin_amdgcn_mfma_f32_16x16x32_bf16(Af, __builtin_bit_cast(bf16x8, B1), acc[1], 0, 0, 0);
      acc[2] = __builtin_amdgcn_mfma_f32_16x16x32_bf16(Af, __builtin_bit_cast(bf16x8, B2), acc[2], 0, 0, 0);
      acc[3] = __builtin_amdgcn_mfma_f32_16x16x32_bf16(Af, __builtin_bit_cast(bf16x8, B3), acc[3], 0, 0, 0);
    }
    if (last) {
      float a[4];
#pragma unroll
      for (int t = 0; t < 4; ++t) {
        float s = wr[0][t] * acc[t][0];
        s = fmaf(wr[1][t], acc[t][1], s);
        s = fmaf(wr[2][t], acc[t][2], s);
        s = fmaf(wr[3][t], acc[t][3], s);
        a[t] = s;
        acc[t] = (f32x4){0.f, 0.f, 0.f, 0.f};
      }
      uint2 pa = make_uint2(pk_bf16(a[0], a[1]), pk_bf16(a[2], a[3]));
      *reinterpret_cast<uint2*>(scr + (nb * 4 + g) * SCR_ROW + li * 8) = pa;
      ++nb;
      if (nb == 4 || !more) {
        __builtin_amdgcn_wave_barrier();
        f32x4 o[4];
#pragma unroll
        for (int t = 0; t < 4; ++t) o[t] = (f32x4){0.f, 0.f, 0.f, 0.f};
#pragma unroll
        for (int s = 0; s < 2; ++s) {
          uint4 bv = *reinterpret_cast<const uint4*>(scr + li * SCR_ROW + (32 * s + 8 * g) * 2);
          bf16x8 Bf = __builtin_bit_cast(bf16x8, bv);
#pragma unroll
          for (int t = 0; t < 4; ++t) {
            bf16x8 Aw = __builtin_bit_cast(bf16x8, S.wo[(s * 4 + t) * 64 + lane]);
            o[t] = __builtin_amdgcn_mfma_f32_16x16x32_bf16(Aw, Bf, o[t], 0, 0, 0);
          }
        }
        if ((li >> 2) < nb) {
          uint4 s0, s1;
          s0.x = pk_bf16(o[0][0], o[0][1]);
          s0.y = pk_bf16(o[0][2], o[0][3]);
          s0.z = pk_bf16(o[1][0], o[1][1]);
          s0.w = pk_bf16(o[1][2], o[1][3]);
          s1.x = pk_bf16(o[2][0], o[2][1]);
          s1.y = pk_bf16(o[2][2], o[2][3]);
          s1.z = pk_bf16(o[3][0], o[3][1]);
          s1.w = pk_bf16(o[3][2], o[3][3]);
          uint16_t* op = out + ((size_t)(node0 + batch0) * 4 + li) * CDIM + 16 * g;
          reinterpret_cast<uint4*>(op)[0] = s0;
          reinterpret_cast<uint4*>(op)[1] = s1;
        }
        __builtin_amdgcn_wave_barrier();
        batch0 += nb;
        nb = 0;
      }
    }
    if (!more) break;
#pragma unroll
    for (int u = 0; u < 8; ++u) cx[u] = nx[u];
    cse0 = nse0;
    cse1 = nse1;
    pos = npos;
    end = nend;
    node = nnode;
  }
}

extern "C" void kernel_launch(void* const* d_in, const int* in_sizes, int n_in, void* d_out, int out_size,
                              void* d_ws, size_t ws_size, hipStream_t stream) {
  const uint16_t* x = (const uint16_t*)d_in[0];
  const int* src = (const int*)d_in[1];
  const int* dst = (const int*)d_in[2];
  const uint2* sh = (const uint2*)d_in[3];
  const uint2* ea = (const uint2*)d_in[4];
  const uint16_t* cg = (const uint16_t*)d_in[5];
  const uint16_t* w_r = (const uint16_t*)d_in[6];
  const uint16_t* w_out = (const uint16_t*)d_in[7];
  uint16_t* out = (uint16_t*)d_out;
  const int N = in_sizes[0] / ROWE;
  const int E = in_sizes[1];

  constexpr int T = 512, CAP = 2048, NBMAX = 128;
  const int NBUCK_TARGET = 512;
  int NB = (N + NBUCK_TARGET - 1) / NBUCK_TARGET;
  if (NB > NBMAX) NB = NBMAX;
  if (NB < 1) NB = 1;
  const int B = (N + NB - 1) / NB;
  const int G = 128;
  int chunk = (E + G - 1) / G;
  chunk = (chunk + 3) & ~3;

  char* ws = (char*)d_ws;
  uint4* rec = (uint4*)ws;
  size_t o = (size_t)E * 32;
  o = (o + 255) & ~(size_t)255;
  int* hist = (int*)(ws + o);
  o += (size_t)G * B * 4;
  o = (o + 255) & ~(size_t)255;
  int* bstart = (int*)(ws + o);

  k_hist<<<G, 1024, B * 4, stream>>>(dst, E, chunk, NB, B, hist);
  k_scatter<<<G, 1024, (2 * B + 16) * 4, stream>>>(src, dst, sh, ea, E, chunk, NB, B, G, hist, bstart, rec);
  typedef MainLds<T, CAP, NBMAX> L;
  hipFuncSetAttribute((const void*)k_main<T, CAP, NBMAX>, hipFuncAttributeMaxDynamicSharedMemorySize,
                      (int)sizeof(L));
  k_main<T, CAP, NBMAX><<<B, T, sizeof(L), stream>>>(x, rec, bstart, cg, w_r, w_out, out, N, NB);
}
```

```cpp
#include <hip/hip_runtime.h>
#include <hip/hip_bf16.h>
#include <stdint.h>

typedef __attribute__((ext_vector_type(8))) short bf16x8;
typedef __attribute__((ext_vector_type(4))) float f32x4;
typedef __attribute__((ext_vector_type(4))) unsigned u32x4;
typedef __attribute__((ext_vector_type(2))) unsigned u32x2;
typedef __attribute__((ext_vector_type(2))) __bf16 bf16x2_t;

#define MDIM 4
#define CDIM 64
#define ROWE 256

static __device__ __forceinline__ unsigned pk_bf16(float a, float b) {
  bf16x2_t v;
  v.x = (__bf16)a;
  v.y = (__bf16)b;
  return __builtin_bit_cast(unsigned, v);
}
static __device__ __forceinline__ float bf_lo(unsigned v) { return __uint_as_float(v << 16); }
static __device__ __forceinline__ float bf_hi(unsigned v) { return __uint_as_float(v & 0xffff0000u); }
static __device__ __forceinline__ int lane_rank(unsigned long long m) {
  return __builtin_amdgcn_mbcnt_hi((unsigned)(m >> 32), __builtin_amdgcn_mbcnt_lo((unsigned)m, 0));
}

template <int NBITS>
static __device__ __forceinline__ unsigned long long match_key(unsigned key, bool act) {
  unsigned long long m = __ballot(act);
#pragma unroll
  for (int i = 0; i < NBITS; ++i) {
    const bool bit = (key >> i) & 1u;
    const unsigned long long bm = __ballot(bit);
    m &= bit ? bm : ~bm;
  }
  return m;
}

template <int T, int Q, int VPT>
__global__ __launch_bounds__(T) void k_hist(const int* __restrict__ dst, int E, int sub, int NB, int B,
                                            int* __restrict__ hist, int* __restrict__ pre, int nbase) {
  extern __shared__ __attribute__((aligned(16))) int s_h[];
  const int tid = threadIdx.x, g = blockIdx.x;
  const unsigned span = (unsigned)B * (unsigned)NB;
  for (int i = tid; i < B; i += T) s_h[i] = 0;
  int4 d[Q][VPT];
  const int nv = sub >> 2;
#pragma unroll
  for (int q = 0; q < Q; ++q) {
    const long lo = (long)(g * Q + q) * sub;
#pragma unroll
    for (int j = 0; j < VPT; ++j) {
      const int i = tid + j * T;
      const long e0 = lo + 4l * i;
      int4 v = make_int4(-1, -1, -1, -1);
      if (i < nv) {
        if (e0 + 3 < E) {
          v = *reinterpret_cast<const int4*>(dst + e0);
        } else {
          if (e0 + 0 < E) v.x = dst[e0 + 0];
          if (e0 + 1 < E) v.y = dst[e0 + 1];
          if (e0 + 2 < E) v.z = dst[e0 + 2];
        }
      }
      d[q][j] = v;
    }
  }
  __syncthreads();
#pragma unroll
  for (int q = 0; q < Q; ++q) {
    if (q > 0) {
      for (int i = tid; i < B; i += T) pre[((size_t)g * Q + q) * B + i] = s_h[i];
      __syncthreads();
    }
#pragma unroll
    for (int j = 0; j < VPT; ++j) {
      const int4 v = d[q][j];
      const unsigned t0 = (unsigned)v.x - (unsigned)nbase, t1 = (unsigned)v.y - (unsigned)nbase;
      const unsigned t2 = (unsigned)v.z - (unsigned)nbase, t3 = (unsigned)v.w - (unsigned)nbase;
      if (v.x >= 0 && t0 < span) atomicAdd(&s_h[t0 / (unsigned)NB], 1);
      if (v.y >= 0 && t1 < span) atomicAdd(&s_h[t1 / (unsigned)NB], 1);
      if (v.z >= 0 && t2 < span) atomicAdd(&s_h[t2 / (unsigned)NB], 1);
      if (v.w >= 0 && t3 < span) atomicAdd(&s_h[t3 / (unsigned)NB], 1);
    }
    __syncthreads();
  }
  for (int i = tid; i < B; i += T) hist[(size_t)g * B + i] = s_h[i];
}

template <int T, int Q, int EPT, bool PACK>
__global__ __launch_bounds__(T) void k_scatter(const int* __restrict__ src, const int* __restrict__ dst,
                                               const u32x2* __restrict__ sh, const u32x2* __restrict__ ea,
                                               int E, int sub, int NB, int B, int G,
                                               const int* __restrict__ hist, const int* __restrict__ pre,
                                               int* __restrict__ bstart, int* __restrict__ recS,
                                               u32x4* __restrict__ recB, unsigned char* __restrict__ recN,
                                               int nbase) {
  extern __shared__ __attribute__((aligned(16))) int s_m[];
  constexpr int CL = T * EPT;
  constexpr int NW = T / 64;
  const int Bp = (B + 63) & ~63;
  const int RG = T / Bp;
  u32x4* stB = reinterpret_cast<u32x4*>(s_m);
  int* stS = s_m + 4 * CL;
  int* stK = s_m + 5 * CL;
  int* s_cw = s_m + 6 * CL;
  int* s_pt = s_cw + NW * B;
  int* s_pp = s_pt + T;
  int* s_base = s_pp + T;
  int* s_loc = s_base + B;
  int* s_ws = s_loc + B;
  const int tid = threadIdx.x, blk = blockIdx.x;
  const int g = blk / Q, q = blk % Q;
  const int lane = tid & 63, wv = tid >> 6;
  const long lo = (long)blk * sub;
  const long hi = min((long)E, lo + sub);
  int ed[EPT], es[EPT];
  u32x2 esh[EPT], eea[EPT];
#pragma unroll
  for (int j = 0; j < EPT; ++j) {
    const long e = lo + wv * (64 * EPT) + j * 64 + lane;
    ed[j] = (e < hi) ? dst[e] : -1;
  }
#pragma unroll
  for (int j = 0; j < EPT; ++j) {
    const unsigned t = (unsigned)ed[j] - (unsigned)nbase;
    ed[j] = (ed[j] >= 0 && t < (unsigned)B * (unsigned)NB) ? (int)t : -1;
  }
  const int col = tid % Bp, rg = tid / Bp;
  const bool tact = rg < RG && col < B;
  int h[16];
#pragma unroll
  for (int j = 0; j < 16; ++j) {
    const int r = rg + j * RG;
    h[j] = (tact && r < G) ? hist[(size_t)r * B + col] : 0;
  }
  int prf0 = 0;
  if (tact && rg == 0 && q > 0) prf0 = pre[((size_t)g * Q + q) * B + col];
#pragma unroll
  for (int j = 0; j < EPT; ++j) {
    const long e = lo + wv * (64 * EPT) + j * 64 + lane;
    es[j] = 0;
    esh[j] = (u32x2){0, 0};
    eea[j] = (u32x2){0, 0};
    if (e < hi) {
      es[j] = src[e];
      esh[j] = sh[e];
      eea[j] = ea[e];
    }
  }
  int* cw = s_cw + wv * B;
  for (int i = lane; i < B; i += 64) cw[i] = 0;
  __builtin_amdgcn_wave_barrier();
  int bk[EPT], rk[EPT];
#pragma unroll
  for (int j = 0; j < EPT; ++j) {
    const bool act = ed[j] >= 0;
    bk[j] = act ? (int)((unsigned)ed[j] / (unsigned)NB) : 0;
    const unsigned long long m = match_key<9>((unsigned)bk[j], act);
    const int r = lane_rank(m);
    const int old = cw[bk[j]];
    rk[j] = old + r;
    __builtin_amdgcn_wave_barrier();
    if (act && r == 0) cw[bk[j]] = old + __popcll(m);
    __builtin_amdgcn_wave_barrier();
  }
  __syncthreads();
  int vl = 0, incl = 0;
  {
    const int b = tid;
    if (b < B) {
      int c[NW];
#pragma unroll
      for (int w = 0; w < NW; ++w) c[w] = s_cw[w * B + b];
#pragma unroll
      for (int w = 0; w < NW; ++w) {
        s_cw[w * B + b] = vl;
        vl += c[w];
      }
    }
    incl = vl;
#pragma unroll
    for (int o = 1; o < 64; o <<= 1) {
      const int tl = __shfl_up(incl, o, 64);
      if (lane >= o) incl += tl;
    }
    if (lane == 63) s_ws[16 + wv] = incl;
  }
  {
    int tot = 0, prf = prf0;
#pragma unroll
    for (int j = 0; j < 16; ++j) {
      const int r = rg + j * RG;
      tot += h[j];
      prf += (r < g) ? h[j] : 0;
    }
    if (tact) {
      for (int r = rg + 16 * RG; r < G; r += RG) {
        const int hv = hist[(size_t)r * B + col];
        tot += hv;
        prf += (r < g) ? hv : 0;
      }
    }
    s_pt[tid] = tot;
    s_pp[tid] = prf;
  }
  __syncthreads();
  int gv = 0, ginc = 0, gprf = 0, nloc = 0;
  {
    const int b = tid;
    int woffL = 0;
#pragma unroll
    for (int w = 0; w < NW; ++w) {
      woffL += (w < wv) ? s_ws[16 + w] : 0;
      nloc += s_ws[16 + w];
    }
    if (b < B) {
      s_loc[b] = woffL + incl - vl;
      for (int r = 0; r < RG; ++r) {
        gv += s_pt[r * Bp + b];
        gprf += s_pp[r * Bp + b];
      }
    }
    ginc = gv;
#pragma unroll
    for (int o = 1; o < 64; o <<= 1) {
      const int t = __shfl_up(ginc, o, 64);
      if (lane >= o) ginc += t;
    }
    if (lane == 63) s_ws[wv] = ginc;
  }
  __syncthreads();
#pragma unroll
  for (int j = 0; j < EPT; ++j) {
    if (ed[j] >= 0) {
      const int p = s_loc[bk[j]] + cw[bk[j]] + rk[j];
      stS[p] = es[j];
      stB[p] = (u32x4){esh[j].x, esh[j].y, eea[j].x, eea[j].y};
      stK[p] = ed[j];
    }
  }
  {
    const int b = tid;
    int woff = 0, total = 0;
#pragma unroll
    for (int w = 0; w < NW; ++w) {
      const int t = s_ws[w];
      woff += (w < wv) ? t : 0;
      total += t;
    }
    const int excl = woff + ginc - gv;
    if (b < B) {
      s_base[b] = excl + gprf;
      if (blk == 0) bstart[b] = excl;
    }
    if (blk == 0 && tid == 0) bstart[B] = total;
  }
  __syncthreads();
  for (int i = tid; i < nloc; i += T) {
    const int d = stK[i];
    const int b = (unsigned)d / (unsigned)NB;
    const size_t o = (size_t)s_base[b] + (i - s_loc[b]);
    const int nl = d - b * NB;
    if (PACK) {
      recS[o] = stS[i] | (nl << 24);
    } else {
      recS[o] = stS[i];
      recN[o] = (unsigned char)nl;
    }
    recB[o] = stB[i];
  }
}

#define SCR_ROW 144
#define SCR_WAVE (16 * SCR_ROW)

template <int T, int CAP, int NBMAX>
struct MainLds {
  u32x4 seS[CAP];
  u32x4 wo[8 * 64];
  f32x4 wrf[4 * 16];
  int srcS[CAP];
  int cw[(T / 64) * NBMAX];
  int off[NBMAX + 8];
  char scr[(T / 64) * SCR_WAVE];
};

#define ISSUE(XV, SE0, SE1, POS, END)                                                     \
  {                                                                                       \
    const int i0_ = (POS) + 2 * g, i1_ = i0_ + 1;                                         \
    const bool v0_ = i0_ < (END), v1_ = i1_ < (END);                                      \
    SE0 = (u32x4){0, 0, 0, 0};                                                            \
    SE1 = (u32x4){0, 0, 0, 0};                                                            \
    _Pragma("unroll") for (int u = 0; u < 8; ++u) XV[u] = (u32x2){0, 0};                  \
    if (v0_) {                                                                            \
      SE0 = S.seS[i0_];                                                                   \
      const int s0_ = S.srcS[i0_];                                                        \
      const u32x2* p_ = reinterpret_cast<const u32x2*>(x + (size_t)s0_ * ROWE + 4 * li);  \
      _Pragma("unroll") for (int u = 0; u < 4; ++u) XV[u] = p_[u * 16];                   \
    }                                                                                     \
    if (v1_) {                                                                            \
      SE1 = S.seS[i1_];                                                                   \
      const int s1_ = S.srcS[i1_];                                                        \
      const u32x2* p_ = reinterpret_cast<const u32x2*>(x + (size_t)s1_ * ROWE + 4 * li);  \
      _Pragma("unroll") for (int u = 0; u < 4; ++u) XV[4 + u] = p_[u * 16];               \
    }                                                                                     \
  }

#define COEF4(SE, C01, C23)                                                               \
  {                                                                                       \
    const float s0_ = bf_lo(SE.x), s1_ = bf_hi(SE.x), s2_ = bf_lo(SE.y), s3_ = bf_hi(SE.y); \
    const unsigned ew_ = (krow & 2) ? SE.w : SE.z;                                        \
    const float ek_ = (krow & 1) ? bf_hi(ew_) : bf_lo(ew_);                               \
    float c_[4];                                                                          \
    _Pragma("unroll") for (int u = 0; u < 4; ++u) {                                       \
      float t_ = cgw[u][0] * s0_;                                                         \
      t_ = fmaf(cgw[u][1], s1_, t_);                                                      \
      t_ = fmaf(cgw[u][2], s2_, t_);                                                      \
      t_ = fmaf(cgw[u][3], s3_, t_);                                                      \
      c_[u] = t_ * ek_;                                                                   \
    }                                                                                     \
    C01 = pk_bf16(c_[0], c_[1]);                                                          \
    C23 = pk_bf16(c_[2], c_[3]);                                                          \
  }

#define COMPUTE(XV, SE0, SE1)                                                             \
  {                                                                                       \
    u32x4 A_;                                                                             \
    COEF4(SE0, A_.x, A_.y);                                                               \
    COEF4(SE1, A_.z, A_.w);                                                               \
    const bf16x8 Af_ = __builtin_bit_cast(bf16x8, A_);                                    \
    u32x4 B0_, B1_, B2_, B3_;                                                             \
    B0_.x = __builtin_amdgcn_perm(XV[1].x, XV[0].x, 0x05040100u);                         \
    B0_.y = __builtin_amdgcn_perm(XV[3].x, XV[2].x, 0x05040100u);                         \
    B0_.z = __builtin_amdgcn_perm(XV[5].x, XV[4].x, 0x05040100u);                         \
    B0_.w = __builtin_amdgcn_perm(XV[7].x, XV[6].x, 0x05040100u);                         \
    B1_.x = __builtin_amdgcn_perm(XV[1].x, XV[0].x, 0x07060302u);                         \
    B1_.y = __builtin_amdgcn_perm(XV[3].x, XV[2].x, 0x07060302u);                         \
    B1_.z = __builtin_amdgcn_perm(XV[5].x, XV[4].x, 0x07060302u);                         \
    B1_.w = __builtin_amdgcn_perm(XV[7].x, XV[6].x, 0x07060302u);                         \
    B2_.x = __builtin_amdgcn_perm(XV[1].y, XV[0].y, 0x05040100u);                         \
    B2_.y = __builtin_amdgcn_perm(XV[3].y, XV[2].y, 0x05040100u);                         \
    B2_.z = __builtin_amdgcn_perm(XV[5].y, XV[4].y, 0x05040100u);                         \
    B2_.w = __builtin_amdgcn_perm(XV[7].y, XV[6].y, 0x05040100u);                         \
    B3_.x = __builtin_amdgcn_perm(XV[1].y, XV[0].y, 0x07060302u);                         \
    B3_.y = __builtin_amdgcn_perm(XV[3].y, XV[2].y, 0x07060302u);                         \
    B3_.z = __builtin_amdgcn_perm(XV[5].y, XV[4].y, 0x07060302u);                         \
    B3_.w = __builtin_amdgcn_perm(XV[7].y, XV[6].y, 0x07060302u);                         \
    acc[0] = __builtin_amdgcn_mfma_f32_16x16x32_bf16(Af_, __builtin_bit_cast(bf16x8, B0_), acc[0], 0, 0, 0); \
    acc[1] = __builtin_amdgcn_mfma_f32_16x16x32_bf16(Af_, __builtin_bit_cast(bf16x8, B1_), acc[1], 0, 0, 0); \
    acc[2] = __builtin_amdgcn_mfma_f32_16x16x32_bf16(Af_, __builtin_bit_cast(bf16x8, B2_), acc[2], 0, 0, 0); \
    acc[3] = __builtin_amdgcn_mfma_f32_16x16x32_bf16(Af_, __builtin_bit_cast(bf16x8, B3_), acc[3], 0, 0, 0); \
  }

#define NODE_FOLD(NB_)                                                                    \
  {                                                                                       \
    float a_[4];                                                                          \
    const f32x4 w0_ = S.wrf[li], w1_ = S.wrf[16 + li], w2_ = S.wrf[32 + li], w3_ = S.wrf[48 + li]; \
    _Pragma("unroll") for (int t = 0; t < 4; ++t) {                                       \
      float s_ = w0_[t] * acc[t][0];                                                      \
      s_ = fmaf(w1_[t], acc[t][1], s_);                                                   \
      s_ = fmaf(w2_[t], acc[t][2], s_);                                                   \
      s_ = fmaf(w3_[t], acc[t][3], s_);                                                   \
      a_[t] = s_;                                                                         \
      acc[t] = (f32x4){0.f, 0.f, 0.f, 0.f};                                               \
    }                                                                                     \
    const u32x2 pa_ = (u32x2){pk_bf16(a_[0], a_[1]), pk_bf16(a_[2], a_[3])};              \
    *reinterpret_cast<u32x2*>(scr + ((NB_)*4 + g) * SCR_ROW + li * 8) = pa_;              \
  }

#define BATCH_OUT(FIRST, NB_)                                                             \
  {                                                                                       \
    __builtin_amdgcn_wave_barrier();                                                      \
    f32x4 o_[4];                                                                          \
    _Pragma("unroll") for (int t = 0; t < 4; ++t) o_[t] = (f32x4){0.f, 0.f, 0.f, 0.f};    \
    _Pragma("unroll") for (int s = 0; s < 2; ++s) {                                       \
      const u32x4 bv_ = *reinterpret_cast<const u32x4*>(scr + li * SCR_ROW + (32 * s + 8 * g) * 2); \
      const bf16x8 Bf_ = __builtin_bit_cast(bf16x8, bv_);                                 \
      _Pragma("unroll") for (int t = 0; t < 4; ++t) {                                     \
        const bf16x8 Aw_ = __builtin_bit_cast(bf16x8, S.wo[(s * 4 + t) * 64 + lane]);     \
        o_[t] = __builtin_amdgcn_mfma_f32_16x16x32_bf16(Aw_, Bf_, o_[t], 0, 0, 0);        \
      }                                                                                   \
    }                                                                                     \
    if ((li >> 2) < (NB_)) {                                                              \
      u32x4 s0_, s1_;                                                                     \
      s0_.x = pk_bf16(o_[0][0], o_[0][1]);                                                \
      s0_.y = pk_bf16(o_[0][2], o_[0][3]);                                                \
      s0_.z = pk_bf16(o_[1][0], o_[1][1]);                                                \
      s0_.w = pk_bf16(o_[1][2], o_[1][3]);                                                \
      s1_.x = pk_bf16(o_[2][0], o_[2][1]);                                                \
      s1_.y = pk_bf16(o_[2][2], o_[2][3]);                                                \
      s1_.z = pk_bf16(o_[3][0], o_[3][1]);                                                \
      s1_.w = pk_bf16(o_[3][2], o_[3][3]);                                                \
      uint16_t* op_ = out + ((size_t)(FIRST)*4 + li) * CDIM + 16 * g;                     \
      reinterpret_cast<u32x4*>(op_)[0] = s0_;                                             \
      reinterpret_cast<u32x4*>(op_)[1] = s1_;                                             \
    }                                                                                     \
    __builtin_amdgcn_wave_barrier();                                                      \
  }

template <int T, int CAP, int NBMAX, bool PACK>
__global__ __launch_bounds__(T, 4) void k_main(const uint16_t* __restrict__ x, const int* __restrict__ recS,
                                               const u32x4* __restrict__ recB,
                                               const unsigned char* __restrict__ recN,
                                               const int* __restrict__ bstart, const uint16_t* __restrict__ cg,
                                               const uint16_t* __restrict__ w_r, const uint16_t* __restrict__ w_out,
                                               uint16_t* __restrict__ out, int N, int NB, int nbase) {
  extern __shared__ __attribute__((aligned(16))) char smem[];
  typedef MainLds<T, CAP, NBMAX> L;
  L& S = *reinterpret_cast<L*>(smem);
  constexpr int NW = T / 64;
  constexpr int RPT = CAP / T;
  const int tid = threadIdx.x, lane = tid & 63;
  const int wv = __builtin_amdgcn_readfirstlane(tid >> 6);
  const int b = blockIdx.x;
  const int node0 = nbase + b * NB;
  const int nn = min(NB, N - node0);
  const int rbeg = __builtin_amdgcn_readfirstlane(bstart[b]);
  const int nrec = __builtin_amdgcn_readfirstlane(bstart[b + 1]) - rbeg;
  const bool slow = nrec > CAP;
  int nlo = (wv * nn) / NW, nhi = ((wv + 1) * nn) / NW;

  {
    int rs[RPT], rn[RPT];
    u32x4 rb[RPT];
#pragma unroll
    for (int q = 0; q < RPT; ++q) {
      const int r = wv * (64 * RPT) + q * 64 + lane;
      rs[q] = 0;
      rb[q] = (u32x4){0, 0, 0, 0};
      rn[q] = -1;
      if (!slow && r < nrec) {
        const size_t o = (size_t)rbeg + r;
        rs[q] = recS[o];
        rb[q] = recB[o];
        if (PACK) {
          rn[q] = (int)((unsigned)rs[q] >> 24);
          rs[q] &= 0xffffff;
        } else {
          rn[q] = recN[o];
        }
      }
    }
    for (int idx = tid; idx < 512; idx += T) {
      int l = idx & 63, st = idx >> 6;
      int s = st >> 2, tp = st & 3;
      int rho = l & 15, gg = l >> 4;
      int d = 16 * (rho >> 2) + 4 * tp + (rho & 3);
      unsigned v[8];
#pragma unroll
      for (int j = 0; j < 8; ++j) v[j] = w_out[(32 * s + 8 * gg + j) * CDIM + d];
      S.wo[idx] = (u32x4){v[0] | (v[1] << 16), v[2] | (v[3] << 16), v[4] | (v[5] << 16), v[6] | (v[7] << 16)};
    }
    if (tid < 64) {
      const int k = tid >> 4, l = tid & 15;
      f32x4 w;
#pragma unroll
      for (int t = 0; t < 4; ++t) w[t] = __uint_as_float((unsigned)w_r[k * CDIM + 4 * l + t] << 16);
      S.wrf[tid] = w;
    }
    int* cw = S.cw + wv * NBMAX;
    for (int i = lane; i < nn; i += 64) cw[i] = 0;
    __builtin_amdgcn_wave_barrier();

    if (!slow) {
      int rk[RPT];
#pragma unroll
      for (int q = 0; q < RPT; ++q) {
        const bool act = rn[q] >= 0;
        const int key = act ? rn[q] : 0;
        const unsigned long long m = match_key<8>((unsigned)key, act);
        const int r = lane_rank(m);
        const int old = cw[key];
        rk[q] = old + r;
        __builtin_amdgcn_wave_barrier();
        if (act && r == 0) cw[key] = old + __popcll(m);
        __builtin_amdgcn_wave_barrier();
      }
      __syncthreads();
      for (int n = tid; n < nn; n += T) {
        int c[NW];
#pragma unroll
        for (int w = 0; w < NW; ++w) c[w] = S.cw[w * NBMAX + n];
        int run = 0;
#pragma unroll
        for (int w = 0; w < NW; ++w) {
          S.cw[w * NBMAX + n] = run;
          run += c[w];
        }
        S.off[n] = run;
      }
      __syncthreads();
      if (wv == 0) {
        int c[4], sum = 0;
#pragma unroll
        for (int j = 0; j < 4; ++j) {
          int i = lane * 4 + j;
          c[j] = (i < nn) ? S.off[i] : 0;
          sum += c[j];
        }
        int inc = sum;
#pragma unroll
        for (int o = 1; o < 64; o <<= 1) {
          int t = __shfl_up(inc, o, 64);
          if (lane >= o) inc += t;
        }
        int run = inc - sum;
#pragma unroll
        for (int j = 0; j < 4; ++j) {
          int i = lane * 4 + j;
          if (i < nn) S.off[i] = run;
          run += c[j];
        }
        if (lane == 63) S.off[nn] = inc;
      }
      __syncthreads();
#pragma unroll
      for (int q = 0; q < RPT; ++q) {
        if (rn[q] >= 0) {
          const int p = S.off[rn[q]] + cw[rn[q]] + rk[q];
          S.srcS[p] = rs[q];
          S.seS[p] = rb[q];
        }
      }
    }
    __syncthreads();
  }

  const int g = lane >> 4, li = lane & 15;
  const int wrow = li >> 2, krow = li & 3;
  char* scr = S.scr + wv * SCR_WAVE;
  if (!slow) {
    const int t0 = (int)(((long)wv * nrec + NW - 1) / NW), t1 = (int)(((long)(wv + 1) * nrec + NW - 1) / NW);
    int c0 = 0, c1 = 0;
    for (int n0 = 0; n0 < nn; n0 += 64) {
      const int n = n0 + lane;
      const int o = (n < nn) ? S.off[n] : 0x7fffffff;
      c0 += __popcll(__ballot(o < t0));
      c1 += __popcll(__ballot(o < t1));
    }
    nlo = (wv == 0) ? 0 : c0;
    nhi = (wv == NW - 1) ? nn : c1;
  }

  if (nlo >= nhi) return;

  float cgw[4][4];
#pragma unroll
  for (int u = 0; u < 4; ++u)
#pragma unroll
    for (int v = 0; v < 4; ++v) cgw[u][v] = __uint_as_float((unsigned)cg[(u * 4 + v) * 4 + wrow] << 16);
  f32x4 acc[4];
#pragma unroll
  for (int t = 0; t < 4; ++t) acc[t] = (f32x4){0.f, 0.f, 0.f, 0.f};

  if (!slow) {
#define NEXT_ITEM(POS, END, NODE)                                                  \
  {                                                                                \
    if (POS + 8 >= END) {                                                          \
      ++NODE;                                                                      \
      POS = END;                                                                   \
      END = (NODE < nhi) ? __builtin_amdgcn_readfirstlane(S.off[NODE + 1]) : POS;  \
    } else {                                                                       \
      POS += 8;                                                                    \
    }                                                                              \
  }
#define ISSUEX(XV, POS, END)                                                              \
  {                                                                                       \
    const int i0_ = (POS) + 2 * g, i1_ = i0_ + 1;                                         \
    _Pragma("unroll") for (int u = 0; u < 8; ++u) XV[u] = (u32x2){0, 0};                  \
    if (i0_ < (END)) {                                                                    \
      const int s0_ = S.srcS[i0_];                                                        \
      const u32x2* p_ = reinterpret_cast<const u32x2*>(x + (size_t)s0_ * ROWE + 4 * li);  \
      _Pragma("unroll") for (int u = 0; u < 4; ++u) XV[u] = p_[u * 16];                   \
    }                                                                                     \
    if (i1_ < (END)) {                                                                    \
      const int s1_ = S.srcS[i1_];                                                        \
      const u32x2* p_ = reinterpret_cast<const u32x2*>(x + (size_t)s1_ * ROWE + 4 * li);  \
      _Pragma("unroll") for (int u = 0; u < 4; ++u) XV[4 + u] = p_[u * 16];               \
    }                                                                                     \
  }
    int node = nlo;
    int pos = __builtin_amdgcn_readfirstlane(S.off[node]);
    int end = __builtin_amdgcn_readfirstlane(S.off[node + 1]);
    int fnode = node, fpos = pos, fend = end;
    u32x2 x0[8], x1[8], x2[8];
    ISSUEX(x0, fpos, fend);
    NEXT_ITEM(fpos, fend, fnode);
    ISSUEX(x1, fpos, fend);
    NEXT_ITEM(fpos, fend, fnode);
    int nb = 0;
    int batch0 = nlo;
#define STEP(XC, XF)                                                               \
  {                                                                                \
    ISSUEX(XF, fpos, fend);                                                        \
    NEXT_ITEM(fpos, fend, fnode);                                                  \
    const int i0_ = pos + 2 * g;                                                   \
    u32x4 se0_ = (u32x4){0, 0, 0, 0}, se1_ = (u32x4){0, 0, 0, 0};                  \
    if (i0_ < end) se0_ = S.seS[i0_];                                              \
    if (i0_ + 1 < end) se1_ = S.seS[i0_ + 1];                                      \
    COMPUTE(XC, se0_, se1_);                                                       \
    if (pos + 8 >= end) {                                                          \
      NODE_FOLD(nb);                                                               \
      ++nb;                                                                        \
      if (nb == 4 || node + 1 >= nhi) {                                            \
        BATCH_OUT(node0 + batch0, nb);                                             \
        batch0 += nb;                                                              \
        nb = 0;                                                                    \
      }                                                                            \
    }                                                                              \
    NEXT_ITEM(pos, end, node);                                                     \
    if (node >= nhi) break;                                                        \
  }
    while (true) {
      STEP(x0, x2);
      STEP(x1, x0);
      STEP(x2, x1);
    }
  } else {
    constexpr int WCAP = CAP / NW;
    const int wbase = wv * WCAP;
    int nb = 0, batch0 = nlo;
    for (int nl = nlo; nl < nhi; ++nl) {
      int cnt = 0;
      for (int base = 0; base < nrec + 64; base += 64) {
        const int r = base + lane;
        int sv = 0, nv = -1;
        if (r < nrec) {
          sv = recS[(size_t)rbeg + r];
          if (PACK) {
            nv = (int)((unsigned)sv >> 24);
            sv &= 0xffffff;
          } else {
            nv = recN[(size_t)rbeg + r];
          }
        }
        const bool m = nv == nl;
        const unsigned long long mask = __ballot(m);
        const int k = __popcll(mask);
        const bool fin = base >= nrec;
        if (cnt + k > WCAP || fin) {
          __builtin_amdgcn_wave_barrier();
          for (int pos = 0; pos < cnt; pos += 8) {
            u32x2 cx[8];
            u32x4 cse0, cse1;
            ISSUE(cx, cse0, cse1, wbase + pos, wbase + cnt);
            COMPUTE(cx, cse0, cse1);
          }
          __builtin_amdgcn_wave_barrier();
          cnt = 0;
        }
        if (m) {
          const int p = wbase + cnt + lane_rank(mask);
          S.srcS[p] = sv;
          S.seS[p] = recB[(size_t)rbeg + r];
        }
        cnt += k;
      }
      NODE_FOLD(nb);
      ++nb;
      if (nb == 4 || nl == nhi - 1) {
        BATCH_OUT(node0 + batch0, nb);
        batch0 += nb;
        nb = 0;
      }
    }
  }
}

extern "C" void kernel_launch(void* const* d_in, const int* in_sizes, int n_in, void* d_out, int out_size,
                              void* d_ws, size_t ws_size, hipStream_t stream) {
  const uint16_t* x = (const uint16_t*)d_in[0];
  const int* src = (const int*)d_in[1];
  const int* dst = (const int*)d_in[2];
  const u32x2* sh = (const u32x2*)d_in[3];
  const u32x2* ea = (const u32x2*)d_in[4];
  const uint16_t* cg = (const uint16_t*)d_in[5];
  const uint16_t* w_r = (const uint16_t*)d_in[6];
  const uint16_t* w_out = (const uint16_t*)d_in[7];
  uint16_t* out = (uint16_t*)d_out;
  const int N = in_sizes[0] / ROWE;
  const int E = in_sizes[1];
  if (N <= 0) return;

  constexpr int T = 1024, CAP = 4 * T, NBMAX = T / 4;
  const int NBUCK_TARGET = 256;
  int NB = (N + NBUCK_TARGET - 1) / NBUCK_TARGET;
  {
    long deg = ((long)E + N - 1) / N;
    if (deg < 1) deg = 1;
    long lim = (CAP * 13 / 16) / deg;
    if (lim < 1) lim = 1;
    if (NB > lim) NB = (int)lim;
  }
  if (NB > NBMAX) NB = NBMAX;
  if (NB < 1) NB = 1;
  const int U = (N + NB - 1) / NB;

  constexpr int PT = 1024, Q = 4, EPT = 4;
  int sub = PT * EPT;
  {
    int want = (E + 255) / 256;
    want = (want + 3) & ~3;
    if (want < 4) want = 4;
    if (want < sub) sub = want;
  }
  const int nsub = (E + sub - 1) / sub;
  const int G = nsub > 0 ? (nsub + Q - 1) / Q : 1;

  constexpr int UMAX = PT / 2;
  const int UP = U < UMAX ? U : UMAX;
  char* ws = (char*)d_ws;
  u32x4* recB = (u32x4*)ws;
  size_t o = ((size_t)E * 16 + 255) & ~(size_t)255;
  int* recS = (int*)(ws + o);
  o += ((size_t)E * 4 + 255) & ~(size_t)255;
  unsigned char* recN = (unsigned char*)(ws + o);
  o += ((size_t)E + 255) & ~(size_t)255;
  int* hist = (int*)(ws + o);
  o += ((size_t)G * UP * 4 + 255) & ~(size_t)255;
  int* pre = (int*)(ws + o);
  o += ((size_t)G * Q * UP * 4 + 255) & ~(size_t)255;
  int* bstart = (int*)(ws + o);

  typedef MainLds<T, CAP, NBMAX> L;
  const int slds = (6 * PT * EPT + (PT / 64) * UP + 2 * PT + 2 * UP + 32) * 4;
  const bool pack = N <= (1 << 24);
  if (pack) {
    (void)hipFuncSetAttribute((const void*)k_scatter<PT, Q, EPT, true>, hipFuncAttributeMaxDynamicSharedMemorySize,
                              slds);
    (void)hipFuncSetAttribute((const void*)k_main<T, CAP, NBMAX, true>, hipFuncAttributeMaxDynamicSharedMemorySize,
                              (int)sizeof(L));
  } else {
    (void)hipFuncSetAttribute((const void*)k_scatter<PT, Q, EPT, false>, hipFuncAttributeMaxDynamicSharedMemorySize,
                              slds);
    (void)hipFuncSetAttribute((const void*)k_main<T, CAP, NBMAX, false>, hipFuncAttributeMaxDynamicSharedMemorySize,
                              (int)sizeof(L));
  }
  for (int u0 = 0; u0 < U; u0 += UMAX) {
    const int Bc = (U - u0) < UMAX ? (U - u0) : UMAX;
    const int nbase = u0 * NB;
    k_hist<1024, Q, (PT * EPT + 4095) / 4096><<<G, 1024, Bc * 4, stream>>>(dst, E, sub, NB, Bc, hist, pre, nbase);
    if (pack) {
      k_scatter<PT, Q, EPT, true><<<G * Q, PT, slds, stream>>>(src, dst, sh, ea, E, sub, NB, Bc, G, hist, pre, bstart,
                                                               recS, recB, recN, nbase);
      k_main<T, CAP, NBMAX, true><<<Bc, T, sizeof(L), stream>>>(x, recS, recB, recN, bstart, cg, w_r, w_out, out,
                                                                N, NB, nbase);
    } else {
      k_scatter<PT, Q, EPT, false><<<G * Q, PT, slds, stream>>>(src, dst, sh, ea, E, sub, NB, Bc, G, hist, pre,
                                                                bstart, recS, recB, recN, nbase);
      k_main<T, CAP, NBMAX, false><<<Bc, T, sizeof(L), stream>>>(x, recS, recB, recN, bstart, cg, w_r, w_out, out,
                                                                 N, NB, nbase);
    }
  }
}
```
